```python
import torch

def _softmax(x, axis=-1):
    return torch.softmax(x, dim=axis)
def _gelu(x):
    return torch.nn.functional.gelu(x, approximate="tanh")
def _silu(x):
    return torch.nn.functional.silu(x)
def _sigmoid(x):
    return torch.sigmoid(x)
def _layernorm(x, axis=-1, eps=1e-5):
    m = x.mean(dim=axis, keepdim=True)
    v = x.var(dim=axis, keepdim=True, unbiased=False)
    return (x - m) / torch.sqrt(v + eps)
def _scatter_add(tgt, idx, src, axis=0):
    return tgt.clone().index_add_(axis, idx, src)
def _segment_sum(src, seg, n):
    out = torch.zeros((n,) + src.shape[1:], dtype=src.dtype)
    return out.index_add_(0, seg, src)
def _pad(x, pw):
    flat = tuple(v for pair in reversed(pw) for v in pair)
    return torch.nn.functional.pad(x, flat)

kernel_name = 'recurrent_scan__sliding_block_cached__mixer_channel_mlp__decode'

def setup_inputs():

    _w = (lambda a: (lambda t: t.to(torch.bfloat16) if t.is_floating_point() else t)(torch.as_tensor(a)))
    def _A_setup():
        B, Sq, D = 1, 1, 512
        Hq, Hkv, Sk, Dh = 32, 32, 32768, 128
        assert Hq % Hkv == 0, (Hq, Hkv)
        return {
            "x": _w((lambda *s: torch.randn(*s, dtype=torch.bfloat16) * 0.1)(B, Sq, D)),
            "k_cache": _w((lambda *s: torch.randn(*s, dtype=torch.bfloat16) * 0.8)(B, Hkv, Sk, Dh)),
            "v_cache": _w((lambda *s: torch.randn(*s, dtype=torch.bfloat16) * 3.2)(B, Hkv, Sk, Dh)),
            "wq": _w((lambda *s: torch.randn(*s, dtype=torch.bfloat16) * 0.8)(D, Hq * Dh) * (D ** -0.5)),
            "wo": _w((lambda *s: torch.randn(*s, dtype=torch.bfloat16) * 3.2)(Hq * Dh, D) * ((Hq * Dh) ** -0.5)),
        }
    _A_in = _A_setup()

    def _B_setup():
        B, S, D, F = 1, 1, 512, 1376
        return {
            "x": _w((lambda *s: torch.randn(*s, dtype=torch.bfloat16) * 0.1)(B, S, D)),
            "w1": _w((lambda *s: torch.randn(*s, dtype=torch.bfloat16) * 3.2)(D, F) * (D ** -0.5)),
            "b1": _w((lambda *s: torch.randn(*s, dtype=torch.bfloat16) * 3.2)(F)),
            "w2": _w((lambda *s: torch.randn(*s, dtype=torch.bfloat16) * 3.2)(F, D) * (F ** -0.5)),
            "b2": _w((lambda *s: torch.randn(*s, dtype=torch.bfloat16) * 3.2)(D)),
        }
    _B_in = _B_setup()

    return {
        "A_x": _A_in["x"],
        "A_k_cache": _A_in["k_cache"],
        "A_v_cache": _A_in["v_cache"],
        "A_wq": _A_in["wq"],
        "A_wo": _A_in["wo"],
        "B_x": _B_in["x"],
        "B_w1": _B_in["w1"],
        "B_b1": _B_in["b1"],
        "B_w2": _B_in["w2"],
        "B_b2": _B_in["b2"],
    }

def reference(A_x, A_k_cache, A_v_cache, A_wq, A_wo, B_x, B_w1, B_b1, B_w2, B_b2):
    def _A_ref(x, k_cache, v_cache, wq, wo):
        B, Sq, D = x.shape
        Hq, Dh = 32, 128
        Hkv, Sk = k_cache.shape[1], k_cache.shape[2]
        W = min(4096, max(256, Sk // 4))
        rep = Hq // Hkv
        q = (lambda x, *d: x.permute(*d))((x @ wq).reshape(B, Sq, Hq, Dh), 0, 2, 1, 3)
        k = (lambda x, n, axis: torch.repeat_interleave(x, n, dim=axis))(k_cache[:, :, Sk - W:, :], rep, axis=1)
        v = (lambda x, n, axis: torch.repeat_interleave(x, n, dim=axis))(v_cache[:, :, Sk - W:, :], rep, axis=1)
        scores = torch.einsum("bhqd,bhkd->bhqk", q, k) * (Dh ** -0.5)
        p = _softmax(scores, axis=-1)
        o = torch.einsum("bhqk,bhkd->bhqd", p, v)
        return (lambda x, *d: x.permute(*d))(o, 0, 2, 1, 3).reshape(B, Sq, Hq * Dh) @ wo

    def _B_ref(x, w1, b1, w2, b2):
        y = _layernorm(x, axis=-1)
        h = _gelu(torch.einsum("bsd,df->bsf", y, w1) + b1)
        return torch.einsum("bsf,fd->bsd", h, w2) + b2

    x = A_x
    xn = _layernorm(x, axis=-1)
    s = _A_ref(xn, A_k_cache, A_v_cache, A_wq, A_wo)
    g = _silu(_B_ref(xn, B_w1, B_b1, B_w2, B_b2))
    return x + s * g
```

```cpp
#include <hip/hip_runtime.h>
#include <hip/hip_bf16.h>
#include <stdint.h>

typedef unsigned short u16;
typedef unsigned u32x4 __attribute__((ext_vector_type(4)));
constexpr int D = 512, H = 32, SK = 32768, DH = 128, W = 4096, F = 1376;
constexpr int NSPLIT = 8, KPS = W / NSPLIT;

__device__ __forceinline__ float bf2f(u16 b) { return __uint_as_float(((unsigned)b) << 16); }
__device__ __forceinline__ float rbf(float f) {
  unsigned u = __float_as_uint(f);
  u += 0x7FFFu + ((u >> 16) & 1u);
  return __uint_as_float(u & 0xFFFF0000u);
}
__device__ __forceinline__ float lo(unsigned u) { return __uint_as_float(u << 16); }
__device__ __forceinline__ float hi(unsigned u) { return __uint_as_float(u & 0xFFFF0000u); }

__device__ float block_sum(float v, float* red) {
  for (int o = 32; o > 0; o >>= 1) v += __shfl_xor(v, o);
  __syncthreads();
  if ((threadIdx.x & 63) == 0) red[threadIdx.x >> 6] = v;
  __syncthreads();
  float s = 0.f;
  for (int i = 0; i < (int)(blockDim.x >> 6); ++i) s += red[i];
  return s;
}
__device__ void ln512(float* xs, float* red) {
  int t = threadIdx.x;
  float a = xs[t], b = xs[t + 256];
  float mean = block_sum(a + b, red) * (1.f / D);
  float da = a - mean, db = b - mean;
  float var = block_sum(da * da + db * db, red) * (1.f / D);
  float m = rbf(mean), v = rbf(var);
  float den = rbf(sqrtf(rbf(v + 1e-5f)));
  __syncthreads();
  xs[t] = rbf(rbf(a - m) / den);
  xs[t + 256] = rbf(rbf(b - m) / den);
  __syncthreads();
}

__global__ void __launch_bounds__(256) k1(const u16* x, const u16* wq, const u16* w1, const u16* b1, float* q, float* hbuf) {
  __shared__ float xs[D]; __shared__ float red[4];
  int t = threadIdx.x;
  xs[t] = bf2f(x[t]); xs[t + 256] = bf2f(x[t + 256]);
  __syncthreads();
  ln512(xs, red);
  if (blockIdx.x < 16) {
    int n = blockIdx.x * 256 + t;
    float acc = 0.f;
    for (int k = 0; k < D; ++k) acc += xs[k] * bf2f(wq[(size_t)k * (H * DH) + n]);
    q[n] = rbf(acc);
  } else {
    ln512(xs, red);
    int n = (blockIdx.x - 16) * 256 + t;
    if (n < F) {
      float acc = 0.f;
      for (int k = 0; k < D; ++k) acc += xs[k] * bf2f(w1[(size_t)k * F + n]);
      float u = rbf(rbf(acc) + bf2f(b1[n]));
      float g = 0.5f * u * (1.f + tanhf(0.7978845608028654f * (u + 0.044715f * u * u * u)));
      hbuf[n] = rbf(g);
    }
  }
}
__global__ void __launch_bounds__(256) k2(const u16* kc, const u16* vc, const float* q, float* po, float* pml) {
  __shared__ float sc[KPS]; __shared__ float red[4]; __shared__ float ored[4][DH];
  int h = blockIdx.x / NSPLIT, j = blockIdx.x % NSPLIT, t = threadIdx.x, lane = t & 63, w = t >> 6;
  const u16* kb = kc + ((size_t)h * SK + (SK - W) + j * KPS) * DH;
  const u16* vb = vc + ((size_t)h * SK + (SK - W) + j * KPS) * DH;
  int c = lane & 15, r = lane >> 4;
  float qv[8];
  for (int i = 0; i < 8; ++i) qv[i] = q[h * DH + c * 8 + i];
  for (int key = w * 4 + r; key < KPS; key += 16) {
    u32x4 kk = *(const u32x4*)(kb + (size_t)key * DH + c * 8);
    float s = 0.f;
    for (int i = 0; i < 4; ++i) s += lo(kk[i]) * qv[2 * i] + hi(kk[i]) * qv[2 * i + 1];
    for (int o = 8; o > 0; o >>= 1) s += __shfl_xor(s, o);
    if (c == 0) sc[key] = rbf(rbf(s) * 0.08838834764831845f);
  }
  __syncthreads();
  float m = fmaxf(sc[t], sc[t + 256]);
  for (int o = 32; o > 0; o >>= 1) m = fmaxf(m, __shfl_xor(m, o));
  if (lane == 0) red[w] = m;
  __syncthreads();
  m = fmaxf(fmaxf(red[0], red[1]), fmaxf(red[2], red[3]));
  float p0 = __expf(sc[t] - m), p1 = __expf(sc[t + 256] - m);
  float l = block_sum(p0 + p1, red);
  sc[t] = p0; sc[t + 256] = p1;
  __syncthreads();
  float acc[8] = {0, 0, 0, 0, 0, 0, 0, 0};
  for (int key = w * 4 + r; key < KPS; key += 16) {
    u32x4 vv = *(const u32x4*)(vb + (size_t)key * DH + c * 8);
    float p = sc[key];
    for (int i = 0; i < 4; ++i) { acc[2 * i] += p * lo(vv[i]); acc[2 * i + 1] += p * hi(vv[i]); }
  }
  for (int i = 0; i < 8; ++i) { acc[i] += __shfl_xor(acc[i], 16); acc[i] += __shfl_xor(acc[i], 32); }
  if (r == 0) for (int i = 0; i < 8; ++i) ored[w][c * 8 + i] = acc[i];
  __syncthreads();
  if (t < DH) po[blockIdx.x * DH + t] = ored[0][t] + ored[1][t] + ored[2][t] + ored[3][t];
  if (t == 0) { pml[blockIdx.x * 2] = m; pml[blockIdx.x * 2 + 1] = l; }
}
__global__ void __launch_bounds__(256) k3(const u16* x, const u16* wo, const u16* w2, const u16* b2, const float* po, const float* pml, const float* hbuf, u16* out) {
  __shared__ float os[H * DH]; __shared__ float red[2][16][16];
  int t = threadIdx.x;
  for (int i = t; i < H * DH; i += 256) {
    int h = i / DH, d = i % DH;
    float M = -INFINITY;
    for (int j = 0; j < NSPLIT; ++j) M = fmaxf(M, pml[(h * NSPLIT + j) * 2]);
    float L = 0.f, o = 0.f;
    for (int j = 0; j < NSPLIT; ++j) { float wgt = __expf(pml[(h * NSPLIT + j) * 2] - M); L += wgt * pml[(h * NSPLIT + j) * 2 + 1]; o += wgt * po[(h * NSPLIT + j) * DH + d]; }
    os[i] = rbf(o / L);
  }
  __syncthreads();
  int c = t & 15, g = t >> 4, n = blockIdx.x * 16 + c;
  float s = 0.f, m2 = 0.f;
  for (int k = g; k < H * DH; k += 16) s += os[k] * bf2f(wo[(size_t)k * D + n]);
  for (int k = g; k < F; k += 16) m2 += hbuf[k] * bf2f(w2[(size_t)k * D + n]);
  red[0][g][c] = s; red[1][g][c] = m2;
  __syncthreads();
  if (t < 16) {
    float ss = 0.f, mm = 0.f;
    for (int i = 0; i < 16; ++i) { ss += red[0][i][t]; mm += red[1][i][t]; }
    int nn = blockIdx.x * 16 + t;
    ss = rbf(ss);
    float u = rbf(rbf(mm) + bf2f(b2[nn]));
    float gg = rbf(u / (1.f + __expf(-u)));
    float o = rbf(bf2f(x[nn]) + rbf(ss * gg));
    out[nn] = (u16)(__float_as_uint(o) >> 16);
  }
}

extern "C" void kernel_launch(void* const* d_in, const int* in_sizes, int n_in, void* d_out, int out_size, void* d_ws, size_t ws_size, hipStream_t stream) {
  const u16* x = (const u16*)d_in[0]; const u16* kc = (const u16*)d_in[1]; const u16* vc = (const u16*)d_in[2];
  const u16* wq = (const u16*)d_in[3]; const u16* wo = (const u16*)d_in[4];
  const u16* w1 = (const u16*)d_in[6]; const u16* b1 = (const u16*)d_in[7]; const u16* w2 = (const u16*)d_in[8]; const u16* b2 = (const u16*)d_in[9];
  float* ws = (float*)d_ws;
  float* q = ws; float* hbuf = ws + 4096; float* po = ws + 8192; float* pml = po + 256 * DH;
  k1<<<22, 256, 0, stream>>>(x, wq, w1, b1, q, hbuf);
  k2<<<256, 256, 0, stream>>>(kc, vc, q, po, pml);
  k3<<<32, 256, 0, stream>>>(x, wo, w2, b2, po, pml, hbuf, (u16*)d_out);
}
```

```cpp
#include <hip/hip_runtime.h>
#include <hip/hip_bf16.h>
#include <stdint.h>

typedef unsigned u32;
typedef unsigned long long u64;
typedef u32 u32x2 __attribute__((ext_vector_type(2)));
typedef u32 u32x4 __attribute__((ext_vector_type(4)));
typedef float f32x2 __attribute__((ext_vector_type(2)));
typedef float f32x4 __attribute__((ext_vector_type(4)));
typedef __bf16 bf16x2 __attribute__((ext_vector_type(2)));
typedef __amdgpu_buffer_rsrc_t rsrc_t;

constexpr int D = 512, SK = 32768, DH = 128, WIN = 4096, FF = 1376, NH = 32;
constexpr int AUX_NT = 2, AUX_SC1 = 16;
constexpr u32 OOB = 0x7FFFFF00u;
constexpr int WS_REC = 2048, WS_BASE = 4096;
constexpr int SQ_OFF = 0, SH_OFF = 256, SM_OFF = 512, S1_OFF = 1024, S2_OFF = 1536;
constexpr u32 SPIN_CAP = 1u << 17;
constexpr float LOG2E = 1.4426950408889634f;

constexpr int KWIN = 4;
constexpr int KPRE = 1, KPRE2 = 3;
constexpr int NV0 = 6;
constexpr int WOTRIG = 10;
constexpr int QSLEEP0 = 40, QSLEEP = 16, S1DLY = 22, S2DLY = 14;
constexpr u32 CODE_BYTES = 10240;

#define SCHED() __builtin_amdgcn_sched_barrier(0)
#define BARRIER() asm volatile("s_waitcnt lgkmcnt(0)\n\ts_barrier" ::: "memory")
#define FENCE() asm volatile("" ::: "memory")
#define WAITVM(n) asm volatile("s_waitcnt vmcnt(%0)" ::"i"(n) : "memory")

__device__ __forceinline__ float asf(u32 u) { return __builtin_bit_cast(float, u); }
__device__ __forceinline__ u32 asu(float f) { return __builtin_bit_cast(u32, f); }
__device__ __forceinline__ float blo(u32 u) { return asf(u << 16); }
__device__ __forceinline__ float bhi(u32 u) { return asf(u & 0xFFFF0000u); }
__device__ __forceinline__ u32 pkbf(float a, float b) {
  bf16x2 r = {(__bf16)a, (__bf16)b};
  return __builtin_bit_cast(u32, r);
}
__device__ __forceinline__ float rbf(float a) { return blo(pkbf(a, a)); }
__device__ __forceinline__ float dot2(u32 a, u32 b, float c) {
  return __builtin_amdgcn_fdot2_f32_bf16(__builtin_bit_cast(bf16x2, a), __builtin_bit_cast(bf16x2, b), c, false);
}
template <int C> __device__ __forceinline__ float dpp(float x) { return asf(__builtin_amdgcn_mov_dpp(asu(x), C, 0xf, 0xf, true)); }
template <int C> __device__ __forceinline__ u32 dppu(u32 x) { return __builtin_amdgcn_mov_dpp(x, C, 0xf, 0xf, true); }
constexpr int X1 = 0xB1, X2 = 0x4E, X7 = 0x141, X15 = 0x140, X8 = 0x128;
__device__ __forceinline__ float sum8(float x) { x += dpp<X1>(x); x += dpp<X2>(x); x += dpp<X7>(x); return x; }
__device__ __forceinline__ float max8(float x) { x = fmaxf(x, dpp<X1>(x)); x = fmaxf(x, dpp<X2>(x)); x = fmaxf(x, dpp<X7>(x)); return x; }
__device__ __forceinline__ float swap16_add(float a, float b) { auto s = __builtin_amdgcn_permlane16_swap(asu(a), asu(b), false, false); return asf(s[0]) + asf(s[1]); }
__device__ __forceinline__ float swap32_add(float a, float b) { auto s = __builtin_amdgcn_permlane32_swap(asu(a), asu(b), false, false); return asf(s[0]) + asf(s[1]); }
__device__ __forceinline__ float sum32(float x) { x = sum8(x); x += dpp<X15>(x); return swap16_add(x, x); }
__device__ __forceinline__ float sum64(float x) { x = sum32(x); return swap32_add(x, x); }
__device__ __forceinline__ float max64(float x) {
  x = max8(x); x = fmaxf(x, dpp<X15>(x));
  auto s = __builtin_amdgcn_permlane16_swap(asu(x), asu(x), false, false); x = fmaxf(asf(s[0]), asf(s[1]));
  auto t = __builtin_amdgcn_permlane32_swap(asu(x), asu(x), false, false); return fmaxf(asf(t[0]), asf(t[1]));
}
__device__ __forceinline__ f32x2 red_rows(const float (&v)[8]) {
  const float u0 = swap32_add(v[0], v[4]), u1 = swap32_add(v[1], v[5]), u2 = swap32_add(v[2], v[6]), u3 = swap32_add(v[3], v[7]);
  return f32x2{swap16_add(u0, u2), swap16_add(u1, u3)};
}
__device__ __forceinline__ float red_rows8(const float (&v)[8], int lane) {
  const f32x2 t = red_rows(v);
  const float a = t.x + dpp<X8>(t.x), b = t.y + dpp<X8>(t.y);
  return (lane & 8) ? b : a;
}
__device__ __forceinline__ rsrc_t mkrsrc(const void* p, u32 bytes) { return __builtin_amdgcn_make_buffer_rsrc((void*)p, 0, bytes, 0x00020000); }
template <int AUX> __device__ __forceinline__ u32x4 ld16(rsrc_t r, u32 voff, u32 soff) { return __builtin_amdgcn_raw_buffer_load_b128(r, voff, soff, AUX); }
__device__ __forceinline__ u32 gcut(float v, u32 t) { return (asu(v) & 0xFFFFFC00u) | t; }
__device__ __forceinline__ bool gbad(u32x4 x) { return (((x.x | x.y) | (x.z | x.w)) & 0x3FFu) != 0; }
__device__ __forceinline__ f32x4 gval(u32x4 x) { return __builtin_bit_cast(f32x4, x); }
__device__ __forceinline__ bool anyl(bool p) { return __builtin_amdgcn_ballot_w64(p) != 0; }
__device__ __forceinline__ void pub64(f32x4 p, u32x2 t2, rsrc_t r, u32 off) {
  __builtin_amdgcn_raw_buffer_store_b64(u32x2{gcut(swap32_add(p.x, p.z), t2.x), gcut(swap32_add(p.y, p.w), t2.y)}, r, off, 0, AUX_SC1);
}
__device__ __forceinline__ f32x4 xsum32(f32x4 v) { return f32x4{swap32_add(v.x, v.x), swap32_add(v.y, v.y), swap32_add(v.z, v.z), swap32_add(v.w, v.w)}; }
__device__ __forceinline__ f32x4 xsum16(f32x4 v) { return f32x4{swap16_add(v.x, v.x), swap16_add(v.y, v.y), swap16_add(v.z, v.z), swap16_add(v.w, v.w)}; }
__device__ __forceinline__ float sigmoid_mul(float u, float z) { return u * __builtin_amdgcn_rcpf(1.f + __builtin_amdgcn_exp2f(-LOG2E * z)); }

__device__ __forceinline__ u32x4 layernorm8(u32x4 X) {
  const float v[8] = {blo(X.x), bhi(X.x), blo(X.y), bhi(X.y), blo(X.z), bhi(X.z), blo(X.w), bhi(X.w)};
  const float mean = sum64(((v[0] + v[1]) + (v[2] + v[3])) + ((v[4] + v[5]) + (v[6] + v[7]))) * (1.f / D);
  float q = 0.f;
#pragma unroll
  for (int i = 0; i < 8; ++i) { const float d = v[i] - mean; q = fmaf(d, d, q); }
  const float var = sum64(q) * (1.f / D);
  const float m = rbf(mean);
  const float inv = __builtin_amdgcn_rcpf(rbf(__builtin_amdgcn_sqrtf(rbf(rbf(var) + 1e-5f))));
  u32x4 o;
#pragma unroll
  for (int i = 0; i < 4; ++i) {
    const u32 d = pkbf(v[2 * i] - m, v[2 * i + 1] - m);
    o[i] = pkbf(blo(d) * inv, bhi(d) * inv);
  }
  return o;
}
__device__ __forceinline__ void rowpair_fma(float (&acc)[8], u32x4 a, u32x4 b, u32 x) {
#pragma unroll
  for (int i = 0; i < 4; ++i) {
    acc[2 * i] = dot2(__builtin_amdgcn_perm(b[i], a[i], 0x05040100u), x, acc[2 * i]);
    acc[2 * i + 1] = dot2(__builtin_amdgcn_perm(b[i], a[i], 0x07060302u), x, acc[2 * i + 1]);
  }
}

template <int SLP> __device__ __forceinline__ u32x4 poll16(rsrc_t r, u32 off, u32x4 T) {
  u32 o0, o1, o2, o3, t, n;
  asm volatile(
      "s_mov_b32 %[n], %[cap]\n"
      "1:\n\t"
      "buffer_load_dwordx4 v[60:63], %[off], %[rs], 0 offen sc1\n\t"
      "s_waitcnt vmcnt(0)\n\t"
      "v_xor_b32 %[o0], v60, %[t0]\n\t"
      "v_xor_b32 %[o1], v61, %[t1]\n\t"
      "v_xor_b32 %[o2], v62, %[t2]\n\t"
      "v_xor_b32 %[o3], v63, %[t3]\n\t"
      "v_or3_b32 %[t], %[o0], %[o1], %[o2]\n\t"
      "v_bitop3_b32 %[t], %[t], %[o3], %[m] bitop3:0xa8\n\t"
      "v_cmp_ne_u32 vcc, 0, %[t]\n\t"
      "s_cbranch_vccz 2f\n\t"
      "s_sleep %[slp]\n\t"
      "s_sub_u32 %[n], %[n], 1\n\t"
      "s_cbranch_scc0 1b\n"
      "2:"
      : [o0] "=&v"(o0), [o1] "=&v"(o1), [o2] "=&v"(o2), [o3] "=&v"(o3), [t] "=&v"(t), [n] "=&s"(n)
      : [off] "v"(off), [rs] "s"(r), [t0] "v"(T.x), [t1] "v"(T.y), [t2] "v"(T.z), [t3] "v"(T.w), [m] "s"(0x3FFu), [cap] "i"(SPIN_CAP), [slp] "i"(SLP)
      : "vcc", "scc", "memory", "v60", "v61", "v62", "v63");
  return u32x4{o0, o1, o2, o3};
}
__device__ __forceinline__ u32x2 wsum8_bf16(f32x4 v, float w) {
  float a, b, c, d; u32 o0, o1;
#define DPP4(CTRL) "v_add_f32_dpp %0, %0, %0 " CTRL " row_mask:0xf bank_mask:0xf\n\tv_add_f32_dpp %1, %1, %1 " CTRL " row_mask:0xf bank_mask:0xf\n\t" \
                   "v_add_f32_dpp %2, %2, %2 " CTRL " row_mask:0xf bank_mask:0xf\n\tv_add_f32_dpp %3, %3, %3 " CTRL " row_mask:0xf bank_mask:0xf\n\t"
  asm("v_mul_f32 %0, %6, %10\n\tv_mul_f32 %1, %7, %10\n\tv_mul_f32 %2, %8, %10\n\tv_mul_f32 %3, %9, %10\n\t"
      DPP4("quad_perm:[1,0,3,2]") DPP4("quad_perm:[2,3,0,1]") DPP4("row_half_mirror")
      "v_cvt_pk_bf16_f32 %4, %0, %1\n\tv_cvt_pk_bf16_f32 %5, %2, %3"
      : "=&v"(a), "=&v"(b), "=&v"(c), "=&v"(d), "=&v"(o0), "=&v"(o1) : "v"(v.x), "v"(v.y), "v"(v.z), "v"(v.w), "v"(w));
#undef DPP4
  return u32x2{o0, o1};
}

typedef __attribute__((address_space(3))) float lds_f32;
typedef __attribute__((address_space(3))) f32x2 lds_f32x2;
#define PIN(x) asm volatile("" : "+v"(x))
struct Lds {
  u32 xs[4][2][256];
  float part[4][128];
  float opart[8][128];
  u32 q[64];
  unsigned short hs[64];
  float ml[8][2];
  u32 tag[4];
  float fw[8];
  u32 flag[64];
};
constexpr int F_P = 0, F_ML = 8, F_VI = 16, F_Q = 24, F_FW = 25;
__device__ __forceinline__ void lds_set(u32* f) { FENCE(); __hip_atomic_store(f, 1u, __ATOMIC_RELAXED, __HIP_MEMORY_SCOPE_WORKGROUP); FENCE(); }
template <int SLEEP> __device__ __forceinline__ void lds_wait(u32* f) {
  for (u32 spin = 0; anyl(__hip_atomic_load(f, __ATOMIC_RELAXED, __HIP_MEMORY_SCOPE_WORKGROUP) == 0) && spin < (SPIN_CAP << 3); ++spin) { FENCE(); if (SLEEP) __builtin_amdgcn_s_sleep(SLEEP); }
  FENCE();
}

__global__ void __launch_bounds__(768, 3) fused_decode(const void* __restrict__ px, const void* __restrict__ pk, const void* __restrict__ pv,
                                                    const void* __restrict__ pwq, const void* __restrict__ pwo, const void* __restrict__ pw1,
                                                    const void* __restrict__ pb1, const void* __restrict__ pw2, const void* __restrict__ pb2,
                                                    void* pout, void* pws) {
  __shared__ __attribute__((aligned(16))) Lds L;
  const int lane = threadIdx.x & 63, wv = __builtin_amdgcn_readfirstlane(threadIdx.x >> 6);
  const int b = blockIdx.x, x8 = b & 7, hq = b >> 6, j = (b >> 3) & 7, h = x8 + 8 * hq;
  u32 pf = 0;
  if (b < 8 && (wv == 1 || wv == 2)) {
    u64 pc;
    asm volatile("s_getpc_b64 %0" : "=s"(pc));
    pf = __builtin_amdgcn_raw_buffer_load_b32(mkrsrc((const void*)(pc & ~(u64)255), CODE_BYTES), (wv - 1) * 8192 + lane * 128, 0, 0);
  }
  const u32 l16 = lane * 16;
  const int a = wv, rr = lane >> 3, cc = lane & 7;
  const int nf8 = x8 < 3 ? 6 : 5;
  const int f0 = 32 * (5 * x8 + (x8 < 3 ? x8 : 3)) + hq * 8 * nf8;
  u32 kvo = (u32)(h * SK + (SK - WIN) + 512 * j) * (DH * 2);
  u32 rot = (u32)(b * 37 + wv) << 10;
  u32 qs = (u32)((128 * (j >> 1) + 32 * a) * 4096 + 128 * h + 64 * (j & 1)) * 2, w1s = (u32)((64 * j + 16 * a) * FF + f0) * 2;
  u32 qo = rr * 32768 + cc * 16, w1o = cc < nf8 ? (u32)(rr * (FF * 4) + cc * 16) : OOB;
  asm volatile("" : "+s"(kvo)); asm volatile("" : "+s"(rot)); asm volatile("" : "+s"(qs)); asm volatile("" : "+s"(w1s));
  asm volatile("" : "+v"(qo), "+v"(w1o));
  SCHED();
  asm volatile("" ::"s"(px), "s"(pk), "s"(pv), "s"(pwq), "s"(pwo), "s"(pw1), "s"(pb1), "s"(pw2), "s"(pb2), "s"(pout), "s"(pws));
  const rsrc_t rws = mkrsrc(pws, WS_BASE + 256 * WS_REC);
  const rsrc_t rk = mkrsrc(pk, NH * SK * DH * 2);
  const bool a0 = a == 0;
#define CH(t) (kvo + ((rot + (t) * 8192u) & 0x1FFFFu))
  u32x4 K[16], X, Q0, Q1, Q2, Q3, WA, WB;
  u32x2 ep = {0u, 0u};
  if (wv < 4) {
    const rsrc_t rq = mkrsrc(pwq, D * NH * DH * 2), r1 = mkrsrc(pw1, D * FF * 2);
    X = ld16<0>(mkrsrc(px, D * 2), l16, 0);
    Q0 = ld16<AUX_NT>(rq, qo, qs); Q1 = ld16<AUX_NT>(rq, qo, qs + 8192); Q2 = ld16<AUX_NT>(rq, qo, qs + 16384); Q3 = ld16<AUX_NT>(rq, qo, qs + 24576);
    WA = ld16<AUX_NT>(r1, w1o, w1s); WB = ld16<AUX_NT>(r1, w1o, w1s + FF * 2);
    if (a0) {
      ep = __builtin_amdgcn_raw_buffer_load_b64(rws, 0, 0, AUX_SC1);
      L.flag[lane] = 0;
    }
  } else {
#pragma unroll
    for (int t = 0; t < KPRE; ++t) K[t] = ld16<AUX_NT>(rk, l16, CH(t));
    if (wv >= 8) {
#pragma unroll
      for (int t = KPRE; t < KPRE2; ++t) K[t] = ld16<AUX_NT>(rk, l16, CH(t));
    }
  }
  SCHED();
  if (wv < 4) asm volatile("s_waitcnt lgkmcnt(0)" ::: "memory");
  asm volatile("s_barrier" ::: "memory");
  if (wv < 4) {
    const u32 grp = WS_BASE + (u32)(x8 + 64 * hq) * WS_REC;
    const u32 me = WS_BASE + (u32)b * WS_REC;
    __builtin_amdgcn_s_setprio(2);
    const u32x4 xn = layernorm8(X);
    *(u32x4*)&L.xs[a][0][4 * lane] = xn;
    *(u32x4*)&L.xs[a][1][4 * lane] = layernorm8(xn);
    {
      const u32x2 xp = *(const u32x2*)&L.xs[a][0][64 * (j >> 1) + 16 * a + 2 * rr];
      const u32 y = L.xs[a][1][32 * j + 8 * a + rr];
      float acc[8] = {0.f, 0.f, 0.f, 0.f, 0.f, 0.f, 0.f, 0.f}, ach[8] = {0.f, 0.f, 0.f, 0.f, 0.f, 0.f, 0.f, 0.f};
      rowpair_fma(acc, Q0, Q1, xp.x);
      rowpair_fma(acc, Q2, Q3, xp.y);
      L.part[a][8 * cc + rr] = red_rows8(acc, lane);
      rowpair_fma(ach, WA, WB, y);
      L.part[a][64 + 8 * cc + rr] = red_rows8(ach, lane);
    }
    lds_set(&L.flag[F_P + a]);
    asm volatile("" : "+v"(pf), "+v"(ep));
    SCHED();
    const int g16 = lane & 15, g32 = lane & 31, hi = lane >> 5;
    const int pi = 2 * x8 + (hq & 1), col = 64 * j + 4 * pi + (lane & 3);
    u64 e1 = 0;
    u32x4 T;
    float xof = 0.f, b2f = 0.f;
    if (a0) {
      e1 = (((u64)__builtin_amdgcn_readfirstlane(ep.y) << 32) | (u32)__builtin_amdgcn_readfirstlane(ep.x)) + 1;
      T = u32x4{(u32)e1 & 0x3FFu, (u32)(e1 >> 10) & 0x3FFu, (u32)(e1 ^ (e1 >> 20)) & 0x3FFu, (u32)((e1 >> 10) ^ (e1 >> 30)) & 0x3FFu};
      lds_wait<0>(&L.flag[F_P + (lane & 3)]);
      {
        const float* pp = &L.part[2 * hi][2 * g32];
        const f32x2 lo = *(const f32x2*)pp + *(const f32x2*)(pp + 128), up = *(const f32x2*)(pp + 64) + *(const f32x2*)(pp + 192);
        pub64(f32x4{lo.x, lo.y, up.x, up.y}, (lane & 1) ? u32x2{T.z, T.w} : u32x2{T.x, T.y}, rws, lane < 56 ? me + SQ_OFF + lane * 8 : OOB);
      }
      *(u32x4*)L.tag = T;
      __builtin_amdgcn_s_setprio(0);
      const u32x2 b1 = __builtin_amdgcn_raw_buffer_load_b64(mkrsrc((const char*)pb1 + f0 * 2, nf8 * 16), g16 * 8, 0, 0);
      const u32 xo = __builtin_amdgcn_raw_buffer_load_b32(mkrsrc(px, D * 2), (col & ~1) * 2, 0, 0), b2 = __builtin_amdgcn_raw_buffer_load_b32(mkrsrc(pb2, D * 2), (col & ~1) * 2, 0, 0);
      __builtin_amdgcn_s_sleep(QSLEEP0);
      const u32 qoff = grp + (2 * hi + (g32 >> 4)) * (8 * WS_REC) + SQ_OFF + g16 * 16;
      const u32 hoff = grp + (lane >> 4) * (8 * WS_REC) + SH_OFF + (g16 < 12 ? g16 : 0) * 16;
      u32x4 g0, g1, g2, g3;
      for (u32 spin = 0;; ++spin) {
        FENCE();
        g0 = ld16<AUX_SC1>(rws, qoff, 0) ^ T; g1 = ld16<AUX_SC1>(rws, qoff, 4 * 8 * WS_REC) ^ T;
        g2 = ld16<AUX_SC1>(rws, hoff, 0) ^ T; g3 = ld16<AUX_SC1>(rws, hoff, 4 * 8 * WS_REC) ^ T;
        if (!anyl(gbad((g0 | g1) | (g2 | g3))) || spin >= SPIN_CAP) break;
        __builtin_amdgcn_s_sleep(QSLEEP);
      }
      const f32x4 qsum = xsum32(gval(g0) + gval(g1));
      *(u32x2*)&L.q[2 * g32] = u32x2{pkbf(qsum.x, qsum.y), pkbf(qsum.z, qsum.w)};
      const f32x4 hsum = xsum32(xsum16(gval(g2) + gval(g3)));
      u32 ge[2];
#pragma unroll
      for (int i = 0; i < 2; ++i) {
        const u32 t = pkbf(hsum[2 * i], hsum[2 * i + 1]), u = pkbf(blo(t) + blo(b1[i]), bhi(t) + bhi(b1[i]));
        const float u0 = blo(u), u1 = bhi(u);
        ge[i] = pkbf(sigmoid_mul(u0, 1.5957691216057308f * fmaf(0.044715f * u0 * u0, u0, u0)), sigmoid_mul(u1, 1.5957691216057308f * fmaf(0.044715f * u1 * u1, u1, u1)));
      }
      *(u32x2*)&L.hs[4 * g16] = g16 < 2 * nf8 ? u32x2{ge[0], ge[1]} : u32x2{0u, 0u};
      lds_set(&L.flag[F_Q]);
      xof = (lane & 1) ? bhi(xo) : blo(xo); b2f = (lane & 1) ? bhi(b2) : blo(b2);
      lds_wait<2>(&L.flag[F_ML + cc]);
      const f32x2 ml = *(const f32x2*)&L.ml[cc][0];
      const float m = max8(ml.x), f = __builtin_amdgcn_exp2f((ml.x - m) * LOG2E), l = sum8(ml.y * f);
      __builtin_amdgcn_raw_buffer_store_b128(u32x4{gcut(m, T.x), gcut(l, T.y), T.z, T.w}, rws, lane < 4 ? me + SM_OFF + l16 : OOB, 0, AUX_SC1);
      L.fw[cc] = f;
      lds_set(&L.flag[F_FW]);
    } else {
      __builtin_amdgcn_s_setprio(0);
      if (a == 1) {
        const u32 t0 = __builtin_amdgcn_raw_buffer_load_b32(rws, lane < 4 ? me + S1_OFF + lane * 128 : OOB, 0, AUX_SC1);
        asm volatile("" ::"v"(t0));
      }
      lds_wait<8>(&L.flag[F_Q]);
      T = *(const u32x4*)L.tag;
    }
    const int nfw = 2 * nf8;
    const rsrc_t ro = mkrsrc(pwo, NH * DH * D * 2), r2 = mkrsrc(pw2, FF * D * 2);
    const u32 os = (u32)((128 * h + 32 * a) * D + 64 * j) * 2, w2s = (u32)((f0 + nfw * a) * D + 64 * j) * 2;
    const u32 oo = rr * 4096 + cc * 16, w2a = rr * 1024 + cc * 16, w2b = rr + 8 < nfw ? w2a + 8192 : OOB;
    const u32 hp = (u32)L.hs[nfw * a + rr] | ((u32)L.hs[nfw * a + 8 + rr] << 16);
    const u32 moff = grp + cc * (8 * WS_REC) + SM_OFF;
    const u32 goff = grp + cc * (8 * WS_REC) + S1_OFF + (8 * a + rr) * 16;
    lds_f32* sw_ = (lds_f32*)&L.part[a][8 * cc + rr];
    lds_f32x2* sp = (lds_f32x2*)&L.part[2 * hi][2 * g32];
    u32 s2off = me + S2_OFF + lane * 8;
    u32x2 t2 = (lane & 1) ? u32x2{T.z, T.w} : u32x2{T.x, T.y};
    PIN(t2);
    u32 foff = WS_BASE + (u32)((g32 & 7) + 64 * (g32 >> 3) + 8 * j) * WS_REC + S2_OFF + (hi * 16 + pi) * 16;
    u32 ooff = lane < 4 ? col * 2 : OOB;
    asm volatile("" : "+v"(sw_), "+v"(sp), "+v"(s2off), "+v"(foff), "+v"(ooff));
    lds_wait<1>(&L.flag[F_VI + cc]);
    u32x4 gm = ld16<AUX_SC1>(rws, moff, 0);
    const u32x4 W2A = ld16<AUX_NT>(r2, w2a, w2s), W2B = ld16<AUX_NT>(r2, w2b, w2s);
    const u32x4 O0 = ld16<AUX_NT>(ro, oo, os), O1 = ld16<AUX_NT>(ro, oo, os + 1024), O2 = ld16<AUX_NT>(ro, oo, os + 2048), O3 = ld16<AUX_NT>(ro, oo, os + 3072);
    SCHED();
    __builtin_amdgcn_s_setprio(2);
    BARRIER();
    SCHED();
    __builtin_amdgcn_s_sleep(S1DLY);
    u32x4 g1st = ld16<AUX_SC1>(rws, goff, 0);
    SCHED();
    gm ^= T;
    if (__builtin_expect(anyl(gbad(gm)), 0)) gm = poll16<2>(rws, moff, T);
    float wg;
    {
      const f32x4 v = gval(gm);
      const float e = __builtin_amdgcn_exp2f((v.x - max8(v.x)) * LOG2E);
      wg = e * __builtin_amdgcn_rcpf(sum8(e * v.y));
    }
    {
      float acc[8] = {0.f, 0.f, 0.f, 0.f, 0.f, 0.f, 0.f, 0.f};
      rowpair_fma(acc, W2A, W2B, hp);
      sw_[64] = red_rows8(acc, lane);
    }
    u32 pw[16];
#pragma unroll
    for (int i = 0; i < 4; ++i) {
      pw[2 * i] = __builtin_amdgcn_perm(O1[i], O0[i], 0x05040100u); pw[2 * i + 1] = __builtin_amdgcn_perm(O1[i], O0[i], 0x07060302u);
      pw[8 + 2 * i] = __builtin_amdgcn_perm(O3[i], O2[i], 0x05040100u); pw[9 + 2 * i] = __builtin_amdgcn_perm(O3[i], O2[i], 0x07060302u);
    }
#pragma unroll
    for (int i = 0; i < 16; ++i) asm volatile("" : "+v"(pw[i]));
    float acc[8] = {0.f, 0.f, 0.f, 0.f, 0.f, 0.f, 0.f, 0.f};
#pragma unroll
    for (int i = 0; i < 8; ++i) PIN(acc[i]);
    PIN(wg);
    SCHED();
    g1st ^= T;
    if (anyl(gbad(g1st))) g1st = poll16<2>(rws, goff, T);
    {
      const u32x2 o = wsum8_bf16(gval(g1st), wg);
#pragma unroll
      for (int i = 0; i < 8; ++i) acc[i] = dot2(pw[8 + i], o.y, dot2(pw[i], o.x, acc[i]));
      sw_[0] = red_rows8(acc, lane);
    }
    BARRIER();
    if (a == 1) {
      const f32x2 lo = sp[0] + sp[64], up = sp[32] + sp[96];
      pub64(f32x4{lo.x, lo.y, up.x, up.y}, t2, rws, s2off);
    }
    if (!a0 || hq >= 2) return;
    const rsrc_t rout = mkrsrc(pout, D * 2);
    const bool l1 = lane & 1, l2 = lane & 2;
    SCHED();
    __builtin_amdgcn_s_sleep(S2DLY);
    const f32x4 gv = gval(poll16<0>(rws, foff, T));
    if (b == 0) __builtin_amdgcn_raw_buffer_store_b64(u32x2{(u32)e1, (u32)(e1 >> 32)}, rws, lane == 0 ? 0u : OOB, 0, AUX_SC1);
    {
      const float s0 = gv.x + dpp<X2>(gv.x), s2 = gv.z + dpp<X2>(gv.z), s1 = gv.y + dpp<X2>(gv.y), s3 = gv.w + dpp<X2>(gv.w);
      const float c0 = l2 ? s2 : s0, c1 = l2 ? s3 : s1;
      const float d0 = c0 + dpp<X1>(c0), d1 = c1 + dpp<X1>(c1);
      float t = l1 ? d1 : d0;
      t += dpp<0x124>(t); t += dpp<X8>(t); t = swap16_add(t, t);
      const u32 tb = asu(rbf(t));
      auto sw = __builtin_amdgcn_permlane32_swap(tb, tb, false, false);
      const float u = rbf(asf(sw[1]) + b2f);
      const float res = xof + rbf(asf(sw[0]) * rbf(sigmoid_mul(u, u)));
      __builtin_amdgcn_raw_buffer_store_b16((unsigned short)pkbf(res, res), rout, ooff, 0, 0);
    }
  } else {
    const int w = wv - 4, r4 = lane >> 4, c = lane & 15;
    const rsrc_t rv = mkrsrc(pv, NH * SK * DH * 2);
#define LDV(t) do { SCHED(); WAITVM(KWIN - 1); V[t] = ld16<AUX_NT>(rv, l16, CH(t)); SCHED(); } while (0)
    u32x4 V[16];
    if (wv < 8) {
#pragma unroll
      for (int t = KPRE; t < KPRE2; ++t) K[t] = ld16<AUX_NT>(rk, l16, CH(t));
    }
    SCHED();
#pragma unroll
    for (int t = KPRE2; t < 16; ++t) { if (t >= KWIN) WAITVM(KWIN - 1);
      K[t] = ld16<AUX_NT>(rk, l16, CH(t)); SCHED(); }
#pragma unroll
    for (int t = 0; t < NV0; ++t) { LDV(t); }
    lds_wait<1>(&L.flag[F_Q]);
    const u32x4 q = *(const u32x4*)&L.q[4 * c];
    float sc[16];
#define QK(t) sc[t] = dot2(K[t].w, q.w, dot2(K[t].z, q.z, dot2(K[t].y, q.y, dot2(K[t].x, q.x, 0.f))));
#define TR(HALF, CTRL) _Pragma("unroll") for (int i = 0; i < (HALF); ++i) { const float lo_ = sc[i] + dpp<CTRL>(sc[i]), hi_ = sc[i + (HALF)] + dpp<CTRL>(sc[i + (HALF)]); sc[i] = (c & (HALF)) ? hi_ : lo_; }
    QK(0) QK(1) QK(2) QK(3) QK(4) QK(5) QK(6) QK(7) QK(8) QK(9) QK(10) QK(11) QK(12) QK(13) QK(14) QK(15)
    LDV(NV0);
    TR(8, X8) TR(4, X7) TR(2, X2) TR(1, X1)
#undef TR
#undef QK
    const float s = rbf(rbf(sc[0]) * 0.08838834764831845f);
    const float mw = max64(s);
    const float p = __builtin_amdgcn_exp2f((s - mw) * LOG2E);
    const u32 plo = pkbf(p, 0.f), phi = plo << 16;
    const float lw = sum64(p);
    *(f32x2*)&L.ml[w][0] = f32x2{mw, lw};
    lds_set(&L.flag[F_ML + w]);
    LDV(NV0 + 1);
    float acc[8] = {0.f, 0.f, 0.f, 0.f, 0.f, 0.f, 0.f, 0.f};
    f32x4 fw = {0.f, 0.f, 0.f, 0.f};
    u32x2 t2 = {0u, 0u};
    lds_f32x2* ow = (lds_f32x2*)&L.opart[w][8 * c + 2 * r4];
    lds_f32x2* op = (lds_f32x2*)&L.opart[4 * (lane >> 5)][2 * (lane & 31)];
    u32 s1off = WS_BASE + b * WS_REC + S1_OFF + lane * 8;
#define PV(t) { const u32 a_ = dppu<0x150 + (t)>(plo), b_ = dppu<0x150 + (t)>(phi); \
      _Pragma("unroll") for (int i = 0; i < 4; ++i) { acc[2 * i] = dot2(V[t][i], a_, acc[2 * i]); acc[2 * i + 1] = dot2(V[t][i], b_, acc[2 * i + 1]); } \
      _Pragma("unroll") for (int i = 0; i < 8; ++i) asm volatile("" : "+v"(acc[i])); \
      if ((t) + NV0 + 2 < 16) LDV(((t) + NV0 + 2) & 15); \
      if ((t) == WOTRIG) lds_set(&L.flag[F_VI + w]); \
      if ((t) == 11 && w == 0) {                                           \
        lds_wait<1>(&L.flag[F_FW]); \
        fw = *(const f32x4*)&L.fw[4 * (lane >> 5)]; \
        t2 = *(const u32x2*)&L.tag[2 * (lane & 1)]; \
        __builtin_amdgcn_s_setprio(3); } \
      if ((t) == 11) asm volatile("" : "+v"(fw), "+v"(t2), "+v"(ow), "+v"(op), "+v"(s1off)); \
      SCHED(); }
    PV(0) PV(1) PV(2) PV(3) PV(4) PV(5) PV(6) PV(7) PV(8) PV(9) PV(10) PV(11) PV(12) PV(13) PV(14) PV(15)
#undef PV
    *ow = red_rows(acc);
    BARRIER();
    if (w == 0) {
      const f32x2 lo = (op[0] * fw.x + op[64] * fw.y) + (op[128] * fw.z + op[192] * fw.w);
      const f32x2 up = (op[32] * fw.x + op[96] * fw.y) + (op[160] * fw.z + op[224] * fw.w);
      pub64(f32x4{lo.x, lo.y, up.x, up.y}, t2, rws, s1off);
    }
  }
}

extern "C" void kernel_launch(void* const* d_in, const int* in_sizes, int n_in, void* d_out, int out_size, void* d_ws, size_t ws_size, hipStream_t stream) {
  fused_decode<<<256, 768, 0, stream>>>(d_in[0], d_in[1], d_in[2], d_in[3], d_in[4], d_in[6], d_in[7], d_in[8], d_in[9], d_out, d_ws);
}
```

```cpp
#include <hip/hip_runtime.h>
#include <hip/hip_bf16.h>
#include <stdint.h>

typedef unsigned u32;
typedef unsigned long long u64;
typedef u32 u32x2 __attribute__((ext_vector_type(2)));
typedef u32 u32x4 __attribute__((ext_vector_type(4)));
typedef float f32x2 __attribute__((ext_vector_type(2)));
typedef float f32x4 __attribute__((ext_vector_type(4)));
typedef __bf16 bf16x2 __attribute__((ext_vector_type(2)));
typedef __amdgpu_buffer_rsrc_t rsrc_t;

constexpr int D = 512, SK = 32768, DH = 128, WIN = 4096, FF = 1376, NH = 32;
constexpr int AUX_NT = 2, AUX_SC1 = 16;
constexpr u32 OOB = 0x7FFFFF00u;
constexpr int WS_REC = 2048, WS_BASE = 4096;
constexpr int SQ_OFF = 0, SH_OFF = 256, SM_OFF = 512, S1_OFF = 1024, S2_OFF = 1536;
constexpr u32 SPIN_CAP = 1u << 17;
constexpr float LOG2E = 1.4426950408889634f;

constexpr int KWIN = 4;
constexpr int KPRE = 1, KPRE2 = 3;
constexpr int NV0 = 6;
constexpr int WOTRIG = 10;
constexpr int QSLEEP0 = 40, QSLEEP = 16, S1DLY = 16, S2DLY = 16;
constexpr u32 CODE_BYTES = 10240;

#define SCHED() __builtin_amdgcn_sched_barrier(0)
#define BARRIER() asm volatile("s_waitcnt lgkmcnt(0)\n\ts_barrier" ::: "memory")
#define FENCE() asm volatile("" ::: "memory")
#define WAITVM(n) asm volatile("s_waitcnt vmcnt(%0)" ::"i"(n) : "memory")

__device__ __forceinline__ float asf(u32 u) { return __builtin_bit_cast(float, u); }
__device__ __forceinline__ u32 asu(float f) { return __builtin_bit_cast(u32, f); }
__device__ __forceinline__ float blo(u32 u) { return asf(u << 16); }
__device__ __forceinline__ float bhi(u32 u) { return asf(u & 0xFFFF0000u); }
__device__ __forceinline__ u32 pkbf(float a, float b) {
  bf16x2 r = {(__bf16)a, (__bf16)b};
  return __builtin_bit_cast(u32, r);
}
__device__ __forceinline__ float rbf(float a) { return blo(pkbf(a, a)); }
__device__ __forceinline__ float dot2(u32 a, u32 b, float c) {
  return __builtin_amdgcn_fdot2_f32_bf16(__builtin_bit_cast(bf16x2, a), __builtin_bit_cast(bf16x2, b), c, false);
}
template <int C> __device__ __forceinline__ float dpp(float x) { return asf(__builtin_amdgcn_mov_dpp(asu(x), C, 0xf, 0xf, true)); }
template <int C> __device__ __forceinline__ u32 dppu(u32 x) { return __builtin_amdgcn_mov_dpp(x, C, 0xf, 0xf, true); }
constexpr int X1 = 0xB1, X2 = 0x4E, X7 = 0x141, X15 = 0x140, X8 = 0x128;
__device__ __forceinline__ float sum8(float x) { x += dpp<X1>(x); x += dpp<X2>(x); x += dpp<X7>(x); return x; }
__device__ __forceinline__ float max8(float x) { x = fmaxf(x, dpp<X1>(x)); x = fmaxf(x, dpp<X2>(x)); x = fmaxf(x, dpp<X7>(x)); return x; }
__device__ __forceinline__ float swap16_add(float a, float b) { auto s = __builtin_amdgcn_permlane16_swap(asu(a), asu(b), false, false); return asf(s[0]) + asf(s[1]); }
__device__ __forceinline__ float swap32_add(float a, float b) { auto s = __builtin_amdgcn_permlane32_swap(asu(a), asu(b), false, false); return asf(s[0]) + asf(s[1]); }
__device__ __forceinline__ float sum32(float x) { x = sum8(x); x += dpp<X15>(x); return swap16_add(x, x); }
__device__ __forceinline__ float sum64(float x) { x = sum32(x); return swap32_add(x, x); }
__device__ __forceinline__ float max64(float x) {
  x = max8(x); x = fmaxf(x, dpp<X15>(x));
  auto s = __builtin_amdgcn_permlane16_swap(asu(x), asu(x), false, false); x = fmaxf(asf(s[0]), asf(s[1]));
  auto t = __builtin_amdgcn_permlane32_swap(asu(x), asu(x), false, false); return fmaxf(asf(t[0]), asf(t[1]));
}
__device__ __forceinline__ f32x2 red_rows(const float (&v)[8]) {
  const float u0 = swap32_add(v[0], v[4]), u1 = swap32_add(v[1], v[5]), u2 = swap32_add(v[2], v[6]), u3 = swap32_add(v[3], v[7]);
  return f32x2{swap16_add(u0, u2), swap16_add(u1, u3)};
}
__device__ __forceinline__ float red_rows8(const float (&v)[8], int lane) {
  const f32x2 t = red_rows(v);
  const float a = t.x + dpp<X8>(t.x), b = t.y + dpp<X8>(t.y);
  return (lane & 8) ? b : a;
}
__device__ __forceinline__ rsrc_t mkrsrc(const void* p, u32 bytes) { return __builtin_amdgcn_make_buffer_rsrc((void*)p, 0, bytes, 0x00020000); }
template <int AUX> __device__ __forceinline__ u32x4 ld16(rsrc_t r, u32 voff, u32 soff) { return __builtin_amdgcn_raw_buffer_load_b128(r, voff, soff, AUX); }
__device__ __forceinline__ u32 gcut(float v, u32 t) { return (asu(v) & 0xFFFFFC00u) | t; }
__device__ __forceinline__ bool gbad(u32x4 x) { return (((x.x | x.y) | (x.z | x.w)) & 0x3FFu) != 0; }
__device__ __forceinline__ f32x4 gval(u32x4 x) { return __builtin_bit_cast(f32x4, x); }
__device__ __forceinline__ bool anyl(bool p) { return __builtin_amdgcn_ballot_w64(p) != 0; }
__device__ __forceinline__ void pub64(f32x4 p, u32x2 t2, rsrc_t r, u32 off) {
  __builtin_amdgcn_raw_buffer_store_b64(u32x2{gcut(swap32_add(p.x, p.z), t2.x), gcut(swap32_add(p.y, p.w), t2.y)}, r, off, 0, AUX_SC1);
}
__device__ __forceinline__ f32x4 xsum32(f32x4 v) { return f32x4{swap32_add(v.x, v.x), swap32_add(v.y, v.y), swap32_add(v.z, v.z), swap32_add(v.w, v.w)}; }
__device__ __forceinline__ f32x4 xsum16(f32x4 v) { return f32x4{swap16_add(v.x, v.x), swap16_add(v.y, v.y), swap16_add(v.z, v.z), swap16_add(v.w, v.w)}; }
__device__ __forceinline__ float sigmoid_mul(float u, float z) { return u * __builtin_amdgcn_rcpf(1.f + __builtin_amdgcn_exp2f(-LOG2E * z)); }

__device__ __forceinline__ u32x4 layernorm8(u32x4 X) {
  const float v[8] = {blo(X.x), bhi(X.x), blo(X.y), bhi(X.y), blo(X.z), bhi(X.z), blo(X.w), bhi(X.w)};
  const float mean = sum64(((v[0] + v[1]) + (v[2] + v[3])) + ((v[4] + v[5]) + (v[6] + v[7]))) * (1.f / D);
  float q = 0.f;
#pragma unroll
  for (int i = 0; i < 8; ++i) { const float d = v[i] - mean; q = fmaf(d, d, q); }
  const float var = sum64(q) * (1.f / D);
  const float m = rbf(mean);
  const float inv = __builtin_amdgcn_rcpf(rbf(__builtin_amdgcn_sqrtf(rbf(rbf(var) + 1e-5f))));
  u32x4 o;
#pragma unroll
  for (int i = 0; i < 4; ++i) {
    const u32 d = pkbf(v[2 * i] - m, v[2 * i + 1] - m);
    o[i] = pkbf(blo(d) * inv, bhi(d) * inv);
  }
  return o;
}
__device__ __forceinline__ void rowpair_fma(float (&acc)[8], u32x4 a, u32x4 b, u32 x) {
#pragma unroll
  for (int i = 0; i < 4; ++i) {
    acc[2 * i] = dot2(__builtin_amdgcn_perm(b[i], a[i], 0x05040100u), x, acc[2 * i]);
    acc[2 * i + 1] = dot2(__builtin_amdgcn_perm(b[i], a[i], 0x07060302u), x, acc[2 * i + 1]);
  }
}

template <int SLP> __device__ __forceinline__ u32x4 poll16(u32x4 x, rsrc_t r, u32 off, u32x4 T) {
  u32 o0 = x.x, o1 = x.y, o2 = x.z, o3 = x.w, t, n; u64 sv;
  asm volatile(
      "s_mov_b32 %[n], %[cap]\n\t"
      "s_mov_b64 %[sv], exec\n"
      "1:\n\t"
      "v_or3_b32 %[t], %[o0], %[o1], %[o2]\n\t"
      "v_bitop3_b32 %[t], %[t], %[o3], %[m] bitop3:0xa8\n\t"
      "v_cmp_ne_u32 vcc, 0, %[t]\n\t"
      "s_cbranch_vccz 2f\n\t"
      "s_sub_u32 %[n], %[n], 1\n\t"
      "s_cbranch_scc1 2f\n\t"
      "s_mov_b64 exec, vcc\n\t"
      "s_sleep %[slp]\n\t"
      "buffer_load_dwordx4 v[60:63], %[off], %[rs], 0 offen sc1\n\t"
      "s_waitcnt vmcnt(0)\n\t"
      "v_xor_b32 %[o0], v60, %[t0]\n\t"
      "v_xor_b32 %[o1], v61, %[t1]\n\t"
      "v_xor_b32 %[o2], v62, %[t2]\n\t"
      "v_xor_b32 %[o3], v63, %[t3]\n\t"
      "s_branch 1b\n"
      "2:\n\t"
      "s_mov_b64 exec, %[sv]"
      : [o0] "+&v"(o0), [o1] "+&v"(o1), [o2] "+&v"(o2), [o3] "+&v"(o3), [t] "=&v"(t), [n] "=&s"(n), [sv] "=&s"(sv)
      : [off] "v"(off), [rs] "s"(r), [t0] "v"(T.x), [t1] "v"(T.y), [t2] "v"(T.z), [t3] "v"(T.w), [m] "s"(0x3FFu), [cap] "i"(SPIN_CAP), [slp] "i"(SLP)
      : "vcc", "scc", "memory", "v60", "v61", "v62", "v63");
  return u32x4{o0, o1, o2, o3};
}
__device__ __forceinline__ u32x2 wsum8_bf16(f32x4 v, float w) {
  float a, b, c, d; u32 o0, o1;
#define DPP4(CTRL) "v_add_f32_dpp %0, %0, %0 " CTRL " row_mask:0xf bank_mask:0xf\n\tv_add_f32_dpp %1, %1, %1 " CTRL " row_mask:0xf bank_mask:0xf\n\t" \
                   "v_add_f32_dpp %2, %2, %2 " CTRL " row_mask:0xf bank_mask:0xf\n\tv_add_f32_dpp %3, %3, %3 " CTRL " row_mask:0xf bank_mask:0xf\n\t"
  asm("v_mul_f32 %0, %6, %10\n\tv_mul_f32 %1, %7, %10\n\tv_mul_f32 %2, %8, %10\n\tv_mul_f32 %3, %9, %10\n\t"
      DPP4("quad_perm:[1,0,3,2]") DPP4("quad_perm:[2,3,0,1]") DPP4("row_half_mirror")
      "v_cvt_pk_bf16_f32 %4, %0, %1\n\tv_cvt_pk_bf16_f32 %5, %2, %3"
      : "=&v"(a), "=&v"(b), "=&v"(c), "=&v"(d), "=&v"(o0), "=&v"(o1) : "v"(v.x), "v"(v.y), "v"(v.z), "v"(v.w), "v"(w));
#undef DPP4
  return u32x2{o0, o1};
}

typedef __attribute__((address_space(3))) float lds_f32;
typedef __attribute__((address_space(3))) f32x2 lds_f32x2;
#define PIN(x) asm volatile("" : "+v"(x))
struct Lds {
  u32 xs[4][2][256];
  float part[4][128];
  float opart[8][128];
  u32 q[64];
  unsigned short hs[64];
  float ml[8][2];
  u32 tag[4];
  float fw[8];
  u32 flag[64];
};
constexpr int F_P = 0, F_ML = 8, F_VI = 16, F_Q = 24, F_FW = 25;
__device__ __forceinline__ void lds_set(u32* f) { FENCE(); __hip_atomic_store(f, 1u, __ATOMIC_RELAXED, __HIP_MEMORY_SCOPE_WORKGROUP); FENCE(); }
template <int SLEEP> __device__ __forceinline__ void lds_wait(u32* f) {
  for (u32 spin = 0; anyl(__hip_atomic_load(f, __ATOMIC_RELAXED, __HIP_MEMORY_SCOPE_WORKGROUP) == 0) && spin < (SPIN_CAP << 3); ++spin) { FENCE(); if (SLEEP) __builtin_amdgcn_s_sleep(SLEEP); }
  FENCE();
}

__global__ void __launch_bounds__(768, 3) fused_decode(const void* __restrict__ px, const void* __restrict__ pk, const void* __restrict__ pv,
                                                    const void* __restrict__ pwq, const void* __restrict__ pwo, const void* __restrict__ pw1,
                                                    const void* __restrict__ pb1, const void* __restrict__ pw2, const void* __restrict__ pb2,
                                                    void* pout, void* pws) {
  __shared__ __attribute__((aligned(16))) Lds L;
  const int lane = threadIdx.x & 63, wv = __builtin_amdgcn_readfirstlane(threadIdx.x >> 6);
  const int b = blockIdx.x, x8 = b & 7, hq = b >> 6, j = (b >> 3) & 7, h = x8 + 8 * hq;
  u32 pf = 0;
  if (b < 8 && (wv == 1 || wv == 2)) {
    u64 pc;
    asm volatile("s_getpc_b64 %0" : "=s"(pc));
    pf = __builtin_amdgcn_raw_buffer_load_b32(mkrsrc((const void*)(pc & ~(u64)255), CODE_BYTES), (wv - 1) * 8192 + lane * 128, 0, 0);
  }
  const u32 l16 = lane * 16;
  const int a = wv, rr = lane >> 3, cc = lane & 7;
  const int nf8 = x8 < 3 ? 6 : 5;
  const int f0 = 32 * (5 * x8 + (x8 < 3 ? x8 : 3)) + hq * 8 * nf8;
  u32 kvo = (u32)(h * SK + (SK - WIN) + 512 * j) * (DH * 2);
  u32 rot = (u32)(b * 37 + wv) << 10;
  u32 qs = (u32)((128 * (j >> 1) + 32 * a) * 4096 + 128 * h + 64 * (j & 1)) * 2, w1s = (u32)((64 * j + 16 * a) * FF + f0) * 2;
  u32 qo = rr * 32768 + cc * 16, w1o = cc < nf8 ? (u32)(rr * (FF * 4) + cc * 16) : OOB;
  asm volatile("" : "+s"(kvo)); asm volatile("" : "+s"(rot)); asm volatile("" : "+s"(qs)); asm volatile("" : "+s"(w1s));
  asm volatile("" : "+v"(qo), "+v"(w1o));
  SCHED();
  asm volatile("" ::"s"(px), "s"(pk), "s"(pv), "s"(pwq), "s"(pwo), "s"(pw1), "s"(pb1), "s"(pw2), "s"(pb2), "s"(pout), "s"(pws));
  const rsrc_t rws = mkrsrc(pws, WS_BASE + 256 * WS_REC);
  const rsrc_t rk = mkrsrc(pk, NH * SK * DH * 2);
  const bool a0 = a == 0;
#define CH(t) (kvo + ((rot + (t) * 8192u) & 0x1FFFFu))
  u32x4 K[16], X, Q0, Q1, Q2, Q3, WA, WB;
  u32x2 ep = {0u, 0u};
  if (wv < 4) {
    const rsrc_t rq = mkrsrc(pwq, D * NH * DH * 2), r1 = mkrsrc(pw1, D * FF * 2);
    X = ld16<0>(mkrsrc(px, D * 2), l16, 0);
    Q0 = ld16<AUX_NT>(rq, qo, qs); Q1 = ld16<AUX_NT>(rq, qo, qs + 8192); Q2 = ld16<AUX_NT>(rq, qo, qs + 16384); Q3 = ld16<AUX_NT>(rq, qo, qs + 24576);
    WA = ld16<AUX_NT>(r1, w1o, w1s); WB = ld16<AUX_NT>(r1, w1o, w1s + FF * 2);
    if (a0) {
      ep = __builtin_amdgcn_raw_buffer_load_b64(rws, 0, 0, AUX_SC1);
      L.flag[lane] = 0;
    }
  } else {
#pragma unroll
    for (int t = 0; t < KPRE; ++t) K[t] = ld16<AUX_NT>(rk, l16, CH(t));
    if (wv >= 8) {
#pragma unroll
      for (int t = KPRE; t < KPRE2; ++t) K[t] = ld16<AUX_NT>(rk, l16, CH(t));
    }
  }
  SCHED();
  if (wv < 4) asm volatile("s_waitcnt lgkmcnt(0)" ::: "memory");
  asm volatile("s_barrier" ::: "memory");
  if (wv < 4) {
    const u32 grp = WS_BASE + (u32)(x8 + 64 * hq) * WS_REC;
    const u32 me = WS_BASE + (u32)b * WS_REC;
    __builtin_amdgcn_s_setprio(2);
    const u32x4 xn = layernorm8(X);
    *(u32x4*)&L.xs[a][0][4 * lane] = xn;
    *(u32x4*)&L.xs[a][1][4 * lane] = layernorm8(xn);
    {
      const u32x2 xp = *(const u32x2*)&L.xs[a][0][64 * (j >> 1) + 16 * a + 2 * rr];
      const u32 y = L.xs[a][1][32 * j + 8 * a + rr];
      float acc[8] = {0.f, 0.f, 0.f, 0.f, 0.f, 0.f, 0.f, 0.f}, ach[8] = {0.f, 0.f, 0.f, 0.f, 0.f, 0.f, 0.f, 0.f};
      rowpair_fma(acc, Q0, Q1, xp.x);
      rowpair_fma(acc, Q2, Q3, xp.y);
      L.part[a][8 * cc + rr] = red_rows8(acc, lane);
      rowpair_fma(ach, WA, WB, y);
      L.part[a][64 + 8 * cc + rr] = red_rows8(ach, lane);
    }
    lds_set(&L.flag[F_P + a]);
    asm volatile("" : "+v"(pf), "+v"(ep));
    SCHED();
    const int g16 = lane & 15, g32 = lane & 31, hi = lane >> 5;
    const int pi = 2 * x8 + (hq & 1), col = 64 * j + 4 * pi + (lane & 3);
    u64 e1 = 0;
    u32x4 T;
    float xof = 0.f, b2f = 0.f;
    if (a0) {
      e1 = (((u64)__builtin_amdgcn_readfirstlane(ep.y) << 32) | (u32)__builtin_amdgcn_readfirstlane(ep.x)) + 1;
      T = u32x4{(u32)e1 & 0x3FFu, (u32)(e1 >> 10) & 0x3FFu, (u32)(e1 ^ (e1 >> 20)) & 0x3FFu, (u32)((e1 >> 10) ^ (e1 >> 30)) & 0x3FFu};
      lds_wait<0>(&L.flag[F_P + (lane & 3)]);
      {
        const float* pp = &L.part[2 * hi][2 * g32];
        const f32x2 lo = *(const f32x2*)pp + *(const f32x2*)(pp + 128), up = *(const f32x2*)(pp + 64) + *(const f32x2*)(pp + 192);
        pub64(f32x4{lo.x, lo.y, up.x, up.y}, (lane & 1) ? u32x2{T.z, T.w} : u32x2{T.x, T.y}, rws, lane < 56 ? me + SQ_OFF + lane * 8 : OOB);
      }
      *(u32x4*)L.tag = T;
      __builtin_amdgcn_s_setprio(0);
      const u32x2 b1 = __builtin_amdgcn_raw_buffer_load_b64(mkrsrc((const char*)pb1 + f0 * 2, nf8 * 16), g16 * 8, 0, 0);
      const u32 xo = __builtin_amdgcn_raw_buffer_load_b32(mkrsrc(px, D * 2), (col & ~1) * 2, 0, 0), b2 = __builtin_amdgcn_raw_buffer_load_b32(mkrsrc(pb2, D * 2), (col & ~1) * 2, 0, 0);
      __builtin_amdgcn_s_sleep(QSLEEP0);
      const u32 qoff = grp + (2 * hi + (g32 >> 4)) * (8 * WS_REC) + SQ_OFF + g16 * 16;
      const u32 hoff = grp + (lane >> 4) * (8 * WS_REC) + SH_OFF + (g16 < 12 ? g16 : 0) * 16;
      u32x4 g0, g1, g2, g3;
      for (u32 spin = 0;; ++spin) {
        FENCE();
        g0 = ld16<AUX_SC1>(rws, qoff, 0) ^ T; g1 = ld16<AUX_SC1>(rws, qoff, 4 * 8 * WS_REC) ^ T;
        g2 = ld16<AUX_SC1>(rws, hoff, 0) ^ T; g3 = ld16<AUX_SC1>(rws, hoff, 4 * 8 * WS_REC) ^ T;
        if (!anyl(gbad((g0 | g1) | (g2 | g3))) || spin >= SPIN_CAP) break;
        __builtin_amdgcn_s_sleep(QSLEEP);
      }
      const f32x4 qsum = xsum32(gval(g0) + gval(g1));
      *(u32x2*)&L.q[2 * g32] = u32x2{pkbf(qsum.x, qsum.y), pkbf(qsum.z, qsum.w)};
      const f32x4 hsum = xsum32(xsum16(gval(g2) + gval(g3)));
      u32 ge[2];
#pragma unroll
      for (int i = 0; i < 2; ++i) {
        const u32 t = pkbf(hsum[2 * i], hsum[2 * i + 1]), u = pkbf(blo(t) + blo(b1[i]), bhi(t) + bhi(b1[i]));
        const float u0 = blo(u), u1 = bhi(u);
        ge[i] = pkbf(sigmoid_mul(u0, 1.5957691216057308f * fmaf(0.044715f * u0 * u0, u0, u0)), sigmoid_mul(u1, 1.5957691216057308f * fmaf(0.044715f * u1 * u1, u1, u1)));
      }
      *(u32x2*)&L.hs[4 * g16] = g16 < 2 * nf8 ? u32x2{ge[0], ge[1]} : u32x2{0u, 0u};
      lds_set(&L.flag[F_Q]);
      xof = (lane & 1) ? bhi(xo) : blo(xo); b2f = (lane & 1) ? bhi(b2) : blo(b2);
      lds_wait<2>(&L.flag[F_ML + cc]);
      const f32x2 ml = *(const f32x2*)&L.ml[cc][0];
      const float m = max8(ml.x), f = __builtin_amdgcn_exp2f((ml.x - m) * LOG2E), l = sum8(ml.y * f);
      __builtin_amdgcn_raw_buffer_store_b128(u32x4{gcut(m, T.x), gcut(l, T.y), T.z, T.w}, rws, lane < 4 ? me + SM_OFF + l16 : OOB, 0, AUX_SC1);
      L.fw[cc] = f;
      lds_set(&L.flag[F_FW]);
    } else {
      __builtin_amdgcn_s_setprio(0);
      if (a == 1) {
        const u32 t0 = __builtin_amdgcn_raw_buffer_load_b32(rws, lane < 4 ? me + S1_OFF + lane * 128 : OOB, 0, AUX_SC1);
        asm volatile("" ::"v"(t0));
      }
      lds_wait<8>(&L.flag[F_Q]);
      T = *(const u32x4*)L.tag;
    }
    const int nfw = 2 * nf8;
    const rsrc_t ro = mkrsrc(pwo, NH * DH * D * 2), r2 = mkrsrc(pw2, FF * D * 2);
    const u32 os = (u32)((128 * h + 32 * a) * D + 64 * j) * 2, w2s = (u32)((f0 + nfw * a) * D + 64 * j) * 2;
    const u32 oo = rr * 4096 + cc * 16, w2a = rr * 1024 + cc * 16, w2b = rr + 8 < nfw ? w2a + 8192 : OOB;
    const u32 hp = (u32)L.hs[nfw * a + rr] | ((u32)L.hs[nfw * a + 8 + rr] << 16);
    const u32 moff = grp + cc * (8 * WS_REC) + SM_OFF;
    const u32 goff = grp + cc * (8 * WS_REC) + S1_OFF + (8 * a + rr) * 16;
    lds_f32* sw_ = (lds_f32*)&L.part[a][8 * cc + rr];
    lds_f32x2* sp = (lds_f32x2*)&L.part[2 * hi][2 * g32];
    u32 s2off = me + S2_OFF + lane * 8;
    u32x2 t2 = (lane & 1) ? u32x2{T.z, T.w} : u32x2{T.x, T.y};
    PIN(t2);
    u32 foff = WS_BASE + (u32)((g32 & 7) + 64 * (g32 >> 3) + 8 * j) * WS_REC + S2_OFF + (hi * 16 + pi) * 16;
    u32 ooff = lane < 4 ? col * 2 : OOB;
    asm volatile("" : "+v"(sw_), "+v"(sp), "+v"(s2off), "+v"(foff), "+v"(ooff));
    lds_wait<1>(&L.flag[F_VI + cc]);
    u32x4 gm = ld16<AUX_SC1>(rws, moff, 0);
    const u32x4 W2A = ld16<AUX_NT>(r2, w2a, w2s), W2B = ld16<AUX_NT>(r2, w2b, w2s);
    const u32x4 O0 = ld16<AUX_NT>(ro, oo, os), O1 = ld16<AUX_NT>(ro, oo, os + 1024), O2 = ld16<AUX_NT>(ro, oo, os + 2048), O3 = ld16<AUX_NT>(ro, oo, os + 3072);
    SCHED();
    __builtin_amdgcn_s_setprio(2);
    BARRIER();
    SCHED();
    __builtin_amdgcn_s_sleep(S1DLY);
    u32x4 g1st = ld16<AUX_SC1>(rws, goff, 0);
    SCHED();
    gm = poll16<1>(gm ^ T, rws, moff, T);
    float wg;
    {
      const f32x4 v = gval(gm);
      const float e = __builtin_amdgcn_exp2f((v.x - max8(v.x)) * LOG2E);
      wg = e * __builtin_amdgcn_rcpf(sum8(e * v.y));
    }
    {
      float acc[8] = {0.f, 0.f, 0.f, 0.f, 0.f, 0.f, 0.f, 0.f};
      rowpair_fma(acc, W2A, W2B, hp);
      sw_[64] = red_rows8(acc, lane);
    }
    u32 pw[16];
#pragma unroll
    for (int i = 0; i < 4; ++i) {
      pw[2 * i] = __builtin_amdgcn_perm(O1[i], O0[i], 0x05040100u); pw[2 * i + 1] = __builtin_amdgcn_perm(O1[i], O0[i], 0x07060302u);
      pw[8 + 2 * i] = __builtin_amdgcn_perm(O3[i], O2[i], 0x05040100u); pw[9 + 2 * i] = __builtin_amdgcn_perm(O3[i], O2[i], 0x07060302u);
    }
#pragma unroll
    for (int i = 0; i < 16; ++i) asm volatile("" : "+v"(pw[i]));
    float acc[8] = {0.f, 0.f, 0.f, 0.f, 0.f, 0.f, 0.f, 0.f};
#pragma unroll
    for (int i = 0; i < 8; ++i) PIN(acc[i]);
    PIN(wg);
    SCHED();
    g1st = poll16<1>(g1st ^ T, rws, goff, T);
    {
      const u32x2 o = wsum8_bf16(gval(g1st), wg);
#pragma unroll
      for (int i = 0; i < 8; ++i) acc[i] = dot2(pw[8 + i], o.y, dot2(pw[i], o.x, acc[i]));
      sw_[0] = red_rows8(acc, lane);
    }
    BARRIER();
    if (a == 1) {
      const f32x2 lo = sp[0] + sp[64], up = sp[32] + sp[96];
      pub64(f32x4{lo.x, lo.y, up.x, up.y}, t2, rws, s2off);
    }
    if (!a0 || hq >= 2) return;
    const rsrc_t rout = mkrsrc(pout, D * 2);
    const bool l1 = lane & 1, l2 = lane & 2;
    SCHED();
    __builtin_amdgcn_s_sleep(S2DLY);
    const f32x4 gv = gval(poll16<0>(u32x4{1u, 1u, 1u, 1u}, rws, foff, T));
    if (b == 0) __builtin_amdgcn_raw_buffer_store_b64(u32x2{(u32)e1, (u32)(e1 >> 32)}, rws, lane == 0 ? 0u : OOB, 0, AUX_SC1);
    {
      const float s0 = gv.x + dpp<X2>(gv.x), s2 = gv.z + dpp<X2>(gv.z), s1 = gv.y + dpp<X2>(gv.y), s3 = gv.w + dpp<X2>(gv.w);
      const float c0 = l2 ? s2 : s0, c1 = l2 ? s3 : s1;
      const float d0 = c0 + dpp<X1>(c0), d1 = c1 + dpp<X1>(c1);
      float t = l1 ? d1 : d0;
      t += dpp<0x124>(t); t += dpp<X8>(t); t = swap16_add(t, t);
      const u32 tb = asu(rbf(t));
      auto sw = __builtin_amdgcn_permlane32_swap(tb, tb, false, false);
      const float u = rbf(asf(sw[1]) + b2f);
      const float res = xof + rbf(asf(sw[0]) * rbf(sigmoid_mul(u, u)));
      __builtin_amdgcn_raw_buffer_store_b16((unsigned short)pkbf(res, res), rout, ooff, 0, 0);
    }
  } else {
    const int w = wv - 4, r4 = lane >> 4, c = lane & 15;
    const rsrc_t rv = mkrsrc(pv, NH * SK * DH * 2);
#define LDV(t) do { SCHED(); WAITVM(KWIN - 1); V[t] = ld16<AUX_NT>(rv, l16, CH(t)); SCHED(); } while (0)
    u32x4 V[16];
    if (wv < 8) {
#pragma unroll
      for (int t = KPRE; t < KPRE2; ++t) K[t] = ld16<AUX_NT>(rk, l16, CH(t));
    }
    SCHED();
#pragma unroll
    for (int t = KPRE2; t < 16; ++t) { if (t >= KWIN) WAITVM(KWIN - 1);
      K[t] = ld16<AUX_NT>(rk, l16, CH(t)); SCHED(); }
#pragma unroll
    for (int t = 0; t < NV0; ++t) { LDV(t); }
    lds_wait<1>(&L.flag[F_Q]);
    const u32x4 q = *(const u32x4*)&L.q[4 * c];
    float sc[16];
#define QK(t) sc[t] = dot2(K[t].w, q.w, dot2(K[t].z, q.z, dot2(K[t].y, q.y, dot2(K[t].x, q.x, 0.f))));
#define TR(HALF, CTRL) _Pragma("unroll") for (int i = 0; i < (HALF); ++i) { const float lo_ = sc[i] + dpp<CTRL>(sc[i]), hi_ = sc[i + (HALF)] + dpp<CTRL>(sc[i + (HALF)]); sc[i] = (c & (HALF)) ? hi_ : lo_; }
    QK(0) QK(1) QK(2) QK(3) QK(4) QK(5) QK(6) QK(7) QK(8) QK(9) QK(10) QK(11) QK(12) QK(13) QK(14) QK(15)
    LDV(NV0);
    TR(8, X8) TR(4, X7) TR(2, X2) TR(1, X1)
#undef TR
#undef QK
    const float s = rbf(rbf(sc[0]) * 0.08838834764831845f);
    const float mw = max64(s);
    const float p = __builtin_amdgcn_exp2f((s - mw) * LOG2E);
    const u32 plo = pkbf(p, 0.f), phi = plo << 16;
    const float lw = sum64(p);
    *(f32x2*)&L.ml[w][0] = f32x2{mw, lw};
    lds_set(&L.flag[F_ML + w]);
    LDV(NV0 + 1);
    float acc[8] = {0.f, 0.f, 0.f, 0.f, 0.f, 0.f, 0.f, 0.f};
    f32x4 fw = {0.f, 0.f, 0.f, 0.f};
    u32x2 t2 = {0u, 0u};
    lds_f32x2* ow = (lds_f32x2*)&L.opart[w][8 * c + 2 * r4];
    lds_f32x2* op = (lds_f32x2*)&L.opart[4 * (lane >> 5)][2 * (lane & 31)];
    u32 s1off = WS_BASE + b * WS_REC + S1_OFF + lane * 8;
#define PV(t) { const u32 a_ = dppu<0x150 + (t)>(plo), b_ = dppu<0x150 + (t)>(phi); \
      _Pragma("unroll") for (int i = 0; i < 4; ++i) { acc[2 * i] = dot2(V[t][i], a_, acc[2 * i]); acc[2 * i + 1] = dot2(V[t][i], b_, acc[2 * i + 1]); } \
      _Pragma("unroll") for (int i = 0; i < 8; ++i) asm volatile("" : "+v"(acc[i])); \
      if ((t) + NV0 + 2 < 16) LDV(((t) + NV0 + 2) & 15); \
      if ((t) == WOTRIG) lds_set(&L.flag[F_VI + w]); \
      if ((t) == 11 && w == 0) {                                           \
        lds_wait<1>(&L.flag[F_FW]); \
        fw = *(const f32x4*)&L.fw[4 * (lane >> 5)]; \
        t2 = *(const u32x2*)&L.tag[2 * (lane & 1)]; \
        __builtin_amdgcn_s_setprio(3); } \
      if ((t) == 11) asm volatile("" : "+v"(fw), "+v"(t2), "+v"(ow), "+v"(op), "+v"(s1off)); \
      SCHED(); }
    PV(0) PV(1) PV(2) PV(3) PV(4) PV(5) PV(6) PV(7) PV(8) PV(9) PV(10) PV(11) PV(12) PV(13) PV(14) PV(15)
#undef PV
    *ow = red_rows(acc);
    BARRIER();
    if (w == 0) {
      const f32x2 lo = (op[0] * fw.x + op[64] * fw.y) + (op[128] * fw.z + op[192] * fw.w);
      const f32x2 up = (op[32] * fw.x + op[96] * fw.y) + (op[160] * fw.z + op[224] * fw.w);
      pub64(f32x4{lo.x, lo.y, up.x, up.y}, t2, rws, s1off);
    }
    asm volatile("s_barrier" ::: "memory");
  }
}

extern "C" void kernel_launch(void* const* d_in, const int* in_sizes, int n_in, void* d_out, int out_size, void* d_ws, size_t ws_size, hipStream_t stream) {
  fused_decode<<<256, 768, 0, stream>>>(d_in[0], d_in[1], d_in[2], d_in[3], d_in[4], d_in[6], d_in[7], d_in[8], d_in[9], d_out, d_ws);
}
```
